```python
import torch

def _softmax(x, axis=-1):
    return torch.softmax(x, dim=axis)
def _gelu(x):
    return torch.nn.functional.gelu(x, approximate="tanh")
def _silu(x):
    return torch.nn.functional.silu(x)
def _sigmoid(x):
    return torch.sigmoid(x)
def _layernorm(x, axis=-1, eps=1e-5):
    m = x.mean(dim=axis, keepdim=True)
    v = x.var(dim=axis, keepdim=True, unbiased=False)
    return (x - m) / torch.sqrt(v + eps)
def _scatter_add(tgt, idx, src, axis=0):
    return tgt.clone().index_add_(axis, idx, src)
def _segment_sum(src, seg, n):
    out = torch.zeros((n,) + src.shape[1:], dtype=src.dtype)
    return out.index_add_(0, seg, src)
def _pad(x, pw):
    flat = tuple(v for pair in reversed(pw) for v in pair)
    return torch.nn.functional.pad(x, flat)

kernel_name = 'standalone__geglu_ffn__decode'

def setup_inputs():

    _w = (lambda a: (lambda t: t.to(torch.bfloat16) if t.is_floating_point() else t)(torch.as_tensor(a)))
    B, S, D, F = 1, 1, 1024, 3584
    return {
        "x": _w((lambda *s: torch.randn(*s, dtype=torch.bfloat16) * 0.05)(B, S, D)),
        "w_gate": _w((lambda *s: torch.randn(*s, dtype=torch.bfloat16) * 0.05)(D, F)),
        "w_up": _w((lambda *s: torch.randn(*s, dtype=torch.bfloat16) * 0.05)(D, F)),
        "w_down": _w((lambda *s: torch.randn(*s, dtype=torch.bfloat16) * 0.05)(F, D)),
    }

def reference(x, w_gate, w_up, w_down):
    return (_gelu(x @ w_gate) * (x @ w_up)) @ w_down
```

```cpp
#include <hip/hip_runtime.h>
#include <hip/hip_bf16.h>

#define GAS __attribute__((address_space(1)))
typedef unsigned v4u __attribute__((ext_vector_type(4)));
typedef unsigned v2u __attribute__((ext_vector_type(2)));
typedef float v4f __attribute__((ext_vector_type(4)));

constexpr int D = 1024, F = 3584;
constexpr int FS = 16;
constexpr int NWG = F / FS;
constexpr int NT = 256;

__device__ __forceinline__ float bflo(unsigned u) { return __builtin_bit_cast(float, u << 16); }
__device__ __forceinline__ float bfhi(unsigned u) { return __builtin_bit_cast(float, u & 0xffff0000u); }
__device__ __forceinline__ float rbf(float f) { return (float)(__bf16)f; }

__device__ __forceinline__ float gelu_tanh(float g) {
    const float k0 = 0.7978845608028654f, k1 = 0.044715f;
    float inner = k0 * (g + k1 * g * g * g);
    return 0.5f * g * (1.0f + tanhf(inner));
}

__global__ void __launch_bounds__(NT) k1(const unsigned short* __restrict__ x, const unsigned short* __restrict__ wg,
                                        const unsigned short* __restrict__ wu, const unsigned short* __restrict__ wd,
                                        float* __restrict__ part) {
    __shared__ float red[4][32];
    __shared__ float hs[16];
    const int tid = threadIdx.x, lane = tid & 63, wave = tid >> 6;
    const int b = blockIdx.x;
    const int slice = (b & 7) * 28 + (b >> 3);
    const int f0 = slice * FS;
    const int half = tid & 1, rg = tid >> 1;
    v4u xv = *(const GAS v4u*)(x + 8 * rg);
    v4u g[8], u[8];
    v2u d[16];
    const GAS unsigned short* pg = (const GAS unsigned short*)wg + (size_t)(8 * rg) * F + f0 + 8 * half;
    const GAS unsigned short* pu = (const GAS unsigned short*)wu + (size_t)(8 * rg) * F + f0 + 8 * half;
#pragma unroll
    for (int i = 0; i < 8; ++i) { g[i] = __builtin_nontemporal_load((const GAS v4u*)(pg + i * F)); u[i] = __builtin_nontemporal_load((const GAS v4u*)(pu + i * F)); }
    const GAS unsigned short* pd = (const GAS unsigned short*)wd + (size_t)f0 * D + 4 * tid;
#pragma unroll
    for (int i = 0; i < 16; ++i) d[i] = __builtin_nontemporal_load((const GAS v2u*)(pd + i * D));
    __builtin_amdgcn_sched_barrier(0);
    float ag[8], au[8];
#pragma unroll
    for (int j = 0; j < 8; ++j) { ag[j] = 0.f; au[j] = 0.f; }
#pragma unroll
    for (int i = 0; i < 8; ++i) {
        float xs = (i & 1) ? bfhi(xv[i >> 1]) : bflo(xv[i >> 1]);
#pragma unroll
        for (int j = 0; j < 4; ++j) {
            ag[2 * j] = fmaf(xs, bflo(g[i][j]), ag[2 * j]);
            ag[2 * j + 1] = fmaf(xs, bfhi(g[i][j]), ag[2 * j + 1]);
            au[2 * j] = fmaf(xs, bflo(u[i][j]), au[2 * j]);
            au[2 * j + 1] = fmaf(xs, bfhi(u[i][j]), au[2 * j + 1]);
        }
    }
#pragma unroll
    for (int j = 0; j < 8; ++j) {
#pragma unroll
        for (int m = 2; m < 64; m <<= 1) { ag[j] += __shfl_xor(ag[j], m); au[j] += __shfl_xor(au[j], m); }
    }
    if (lane < 2) {
#pragma unroll
        for (int j = 0; j < 8; ++j) { red[wave][half * 8 + j] = ag[j]; red[wave][16 + half * 8 + j] = au[j]; }
    }
    __syncthreads();
    if (tid < 16) {
        float gs = red[0][tid] + red[1][tid] + red[2][tid] + red[3][tid];
        float us = red[0][16 + tid] + red[1][16 + tid] + red[2][16 + tid] + red[3][16 + tid];
        gs = rbf(gs); us = rbf(us);
        float a = rbf(gelu_tanh(gs));
        hs[tid] = rbf(a * us);
    }
    __syncthreads();
    v4f o = {0.f, 0.f, 0.f, 0.f};
#pragma unroll
    for (int i = 0; i < 16; ++i) {
        float h = hs[i];
        o[0] = fmaf(h, bflo(d[i][0]), o[0]);
        o[1] = fmaf(h, bfhi(d[i][0]), o[1]);
        o[2] = fmaf(h, bflo(d[i][1]), o[2]);
        o[3] = fmaf(h, bfhi(d[i][1]), o[3]);
    }
    *(GAS v4f*)((GAS float*)part + (size_t)slice * D + 4 * tid) = o;
}

__global__ void __launch_bounds__(256) k2(const float* __restrict__ part, __hip_bfloat16* __restrict__ out) {
    __shared__ v4f red[4][4];
    const int tid = threadIdx.x, lane = tid & 63, wave = tid >> 6;
    const int c4 = tid & 3, p = tid >> 2;
    const int col = blockIdx.x * 16 + c4 * 4;
    v4f s = {0.f, 0.f, 0.f, 0.f};
    v4f v[4];
#pragma unroll
    for (int i = 0; i < 4; ++i) {
        int pp = p + 64 * i;
        v[i] = (pp < NWG) ? *(const GAS v4f*)((const GAS float*)part + (size_t)pp * D + col) : s;
    }
    s = (v[0] + v[1]) + (v[2] + v[3]);
#pragma unroll
    for (int m = 4; m < 64; m <<= 1) {
#pragma unroll
        for (int j = 0; j < 4; ++j) s[j] += __shfl_xor(s[j], m);
    }
    if (lane < 4) red[wave][lane] = s;
    __syncthreads();
    if (tid < 16) {
        float t = red[0][tid >> 2][tid & 3] + red[1][tid >> 2][tid & 3] + red[2][tid >> 2][tid & 3] + red[3][tid >> 2][tid & 3];
        ((__bf16*)out)[blockIdx.x * 16 + tid] = (__bf16)t;
    }
}

extern "C" void kernel_launch(void* const* d_in, const int* in_sizes, int n_in, void* d_out, int out_size, void* d_ws,
                              size_t ws_size, hipStream_t stream) {
    const unsigned short* x = (const unsigned short*)d_in[0];
    const unsigned short* wg = (const unsigned short*)d_in[1];
    const unsigned short* wu = (const unsigned short*)d_in[2];
    const unsigned short* wd = (const unsigned short*)d_in[3];
    float* part = (float*)d_ws;
    k1<<<NWG, NT, 0, stream>>>(x, wg, wu, wd, part);
    k2<<<64, 256, 0, stream>>>(part, (__hip_bfloat16*)d_out);
}
```

```cpp
#include <hip/hip_runtime.h>
#include <hip/hip_bf16.h>

#define GAS __attribute__((address_space(1)))
typedef unsigned v4u __attribute__((ext_vector_type(4)));
typedef unsigned v2u __attribute__((ext_vector_type(2)));
typedef float v4f __attribute__((ext_vector_type(4)));
typedef unsigned long long u64;
typedef GAS u64 gu64;
#define RLX_AGENT __ATOMIC_RELAXED, __HIP_MEMORY_SCOPE_AGENT

constexpr int D = 1024, F = 3584;
constexpr int FS = 32;
constexpr int NWG = F / FS;
constexpr int NT = 1024;
constexpr int NCONS = 64;
constexpr u64 TAG_STEP = 0x9E3779B97Full;

__device__ __forceinline__ float bflo(unsigned u) { return __builtin_bit_cast(float, u << 16); }
__device__ __forceinline__ float bfhi(unsigned u) { return __builtin_bit_cast(float, u & 0xffff0000u); }
__device__ __forceinline__ float rbf(float f) { return (float)(__bf16)f; }
__device__ __forceinline__ float gelu_tanh(float g) {
    const float k0 = 0.7978845608028654f, k1 = 0.044715f;
    float inner = k0 * (g + k1 * g * g * g);
    return g / (1.0f + __expf(-2.0f * inner));
}
__device__ __forceinline__ v4u ldnt(const GAS unsigned short* p) { return __builtin_nontemporal_load((const GAS v4u*)p); }

__global__ void __launch_bounds__(NT) kf(const unsigned short* __restrict__ x, const unsigned short* __restrict__ wg,
                                        const unsigned short* __restrict__ wu, const unsigned short* __restrict__ wd,
                                        u64* ws, __hip_bfloat16* __restrict__ out) {
    __shared__ float red[8 * 1024];
    __shared__ float hs[32];
    const int tid = threadIdx.x, lane = tid & 63, wave = tid >> 6;
    const int b = blockIdx.x;
    const int slice = (b & 7) * 14 + (b >> 3);
    const int f0 = slice * FS;
    const int c = tid & 3, r = tid >> 2;
    v4u g[4], u[4], d[4];
    const GAS unsigned short* pg = (const GAS unsigned short*)wg + (size_t)(4 * r) * F + f0 + 8 * c;
    const GAS unsigned short* pu = (const GAS unsigned short*)wu + (size_t)(4 * r) * F + f0 + 8 * c;
    v2u xv = *(const GAS v2u*)(x + 4 * r);
    u64 ep = __hip_atomic_load((gu64*)ws, RLX_AGENT);
#pragma unroll
    for (int i = 0; i < 4; ++i) { g[i] = ldnt(pg + i * F); u[i] = ldnt(pu + i * F); }
    const int c8 = tid & 127, rg = tid >> 7;
    const GAS unsigned short* pd = (const GAS unsigned short*)wd + (size_t)(f0 + 4 * rg) * D + 8 * c8;
#pragma unroll
    for (int i = 0; i < 4; ++i) d[i] = ldnt(pd + i * D);
    __builtin_amdgcn_sched_barrier(0);
    float a[16];
#pragma unroll
    for (int j = 0; j < 16; ++j) a[j] = 0.f;
#pragma unroll
    for (int i = 0; i < 4; ++i) {
        float xs = (i & 1) ? bfhi(xv[i >> 1]) : bflo(xv[i >> 1]);
#pragma unroll
        for (int j = 0; j < 4; ++j) {
            a[2 * j] = fmaf(xs, bflo(g[i][j]), a[2 * j]);
            a[2 * j + 1] = fmaf(xs, bfhi(g[i][j]), a[2 * j + 1]);
            a[8 + 2 * j] = fmaf(xs, bflo(u[i][j]), a[8 + 2 * j]);
            a[8 + 2 * j + 1] = fmaf(xs, bfhi(u[i][j]), a[8 + 2 * j + 1]);
        }
    }
#pragma unroll
    for (int j = 0; j < 16; ++j) {
#pragma unroll
        for (int m = 4; m < 64; m <<= 1) a[j] += __shfl_xor(a[j], m);
    }
    if (lane < 4) {
#pragma unroll
        for (int j = 0; j < 8; ++j) { red[wave * 64 + c * 8 + j] = a[j]; red[wave * 64 + 32 + c * 8 + j] = a[8 + j]; }
    }
    __syncthreads();
    if (tid < 32) {
        float gs = 0.f, us = 0.f;
#pragma unroll
        for (int w = 0; w < 16; ++w) { gs += red[w * 64 + tid]; us += red[w * 64 + 32 + tid]; }
        gs = rbf(gs); us = rbf(us);
        float act = rbf(gelu_tanh(gs));
        hs[tid] = rbf(act * us);
    }
    __syncthreads();
    float o[8];
#pragma unroll
    for (int j = 0; j < 8; ++j) o[j] = 0.f;
#pragma unroll
    for (int i = 0; i < 4; ++i) {
        float h = hs[4 * rg + i];
#pragma unroll
        for (int j = 0; j < 4; ++j) { o[2 * j] = fmaf(h, bflo(d[i][j]), o[2 * j]); o[2 * j + 1] = fmaf(h, bfhi(d[i][j]), o[2 * j + 1]); }
    }
    *(v4f*)&red[rg * 1024 + 8 * c8] = v4f{o[0], o[1], o[2], o[3]};
    *(v4f*)&red[rg * 1024 + 8 * c8 + 4] = v4f{o[4], o[5], o[6], o[7]};
    __syncthreads();
    float s = 0.f;
#pragma unroll
    for (int k = 0; k < 8; ++k) s += red[k * 1024 + tid];
    const u64 tag = ((ep >> 24) + TAG_STEP) & 0xFFFFFFFFFFull;
    gu64* gr = (gu64*)ws + 64;
    {
        unsigned sb = __builtin_bit_cast(unsigned, s);
        sb = (sb + 0x80u) >> 8;
        __hip_atomic_store(gr + (size_t)slice * 1024 + tid, (tag << 24) | sb, RLX_AGENT);
    }
    if (b < NCONS) {
        const int cc = tid & 15, p = tid >> 4;
        gu64* q0 = gr + (size_t)p * 1024 + b * 16 + cc;
        gu64* q1 = gr + (size_t)(p < 48 ? p + 64 : p) * 1024 + b * 16 + cc;
        u64 v0, v1;
        for (unsigned spins = 0; spins < (1u << 20); ++spins) {
            v0 = __hip_atomic_load(q0, RLX_AGENT);
            v1 = __hip_atomic_load(q1, RLX_AGENT);
            if (__all((v0 >> 24) == tag && (v1 >> 24) == tag)) break;
        }
        float f = __builtin_bit_cast(float, (unsigned)v0 << 8);
        if (p < 48) f += __builtin_bit_cast(float, (unsigned)v1 << 8);
        f += __shfl_xor(f, 16); f += __shfl_xor(f, 32);
        __syncthreads();
        if (lane < 16) red[wave * 16 + lane] = f;
        __syncthreads();
        if (tid < 16) {
            float t = 0.f;
#pragma unroll
            for (int w = 0; w < 16; ++w) t += red[w * 16 + tid];
            ((__bf16*)out)[b * 16 + tid] = (__bf16)t;
        }
        if (b == 0 && tid == 0) __hip_atomic_store((gu64*)ws, tag << 24, RLX_AGENT);
    }
}

extern "C" void kernel_launch(void* const* d_in, const int* in_sizes, int n_in, void* d_out, int out_size, void* d_ws,
                              size_t ws_size, hipStream_t stream) {
    kf<<<NWG, NT, 0, stream>>>((const unsigned short*)d_in[0], (const unsigned short*)d_in[1], (const unsigned short*)d_in[2],
                               (const unsigned short*)d_in[3], (u64*)d_ws, (__hip_bfloat16*)d_out);
}
```

```cpp
#include <hip/hip_runtime.h>

__global__ void __launch_bounds__(512, 4) __attribute__((aligned(4096)))
geglu_ffn_decode(const void* x, const void* w_gate, const void* w_up, const void* w_down, void* ws, void* out) {
    __shared__ __attribute__((aligned(16))) unsigned lds[1028];
    asm volatile(
        "s_load_dwordx8 s[20:27], s[0:1], 0\n"
        "s_load_dwordx4 s[28:31], s[0:1], 32\n"
        "v_readfirstlane_b32 s3, v0\n"
        "v_mov_b32 v16, 0\n"
        "s_and_b32 s13, s2, 7\n"
        "s_mul_i32 s13, s13, 28\n"
        "s_lshr_b32 s14, s2, 3\n"
        "s_add_u32 s13, s13, s14\n"
        "ds_write_b32 v16, v16 offset:4096\n"
        "s_lshr_b32 s3, s3, 6\n"
        "s_lshr_b32 s12, s13, 3\n"
        "s_and_b32 s13, s13, 7\n"
        "s_mov_b32 s18, 7340032\n"
        "s_mov_b32 s19, 0x20000\n"
        "v_and_b32 v18, 15, v0\n"
        "s_and_b32 s15, s3, 3\n"
        "s_mul_i32 s15, s15, 50\n"
        "s_movk_i32 s58, 200\n"
        "s_getpc_b64 s[62:63]\n"
        ".Lgeglu_ffn_decode_pc:\n"
        "s_add_u32 s62, s62, .Lgeglu_ffn_decode_pace-.Lgeglu_ffn_decode_pc\n"
        "s_addc_u32 s63, s63, 0\n"
        "s_cmpk_gt_u32 s2, 223\n"
        "s_cbranch_scc1 .Lgeglu_ffn_decode_service\n"
        "s_lshr_b32 s14, s13, 1\n"
        "v_lshrrev_b32 v2, 4, v0\n"
        "v_mul_u32_u24 v1, 7168, v2\n"
        "s_mul_i32 s36, s14, 114688\n"
        "s_lshl_b32 s37, s12, 8\n"
        "v_lshl_add_u32 v1, v18, 4, v1\n"
        "s_add_u32 s36, s36, s37\n"
        "s_lshl_b32 s37, s14, 5\n"
        "v_lshl_add_u32 v2, v2, 1, s37\n"
        "v_lshl_add_u32 v2, v18, 7, v2\n"
        "s_mov_b32 s35, 458752\n"
        "s_bitcmp1_b32 s2, 3\n"
        "s_waitcnt lgkmcnt(0)\n"
        "s_cselect_b64 s[16:17], s[24:25], s[22:23]\n"
        "s_cmp_gt_u32 s3, 3\n"
        "s_memrealtime s[48:49]\n"
        "s_cbranch_scc1 .Lgeglu_ffn_decode_down\n"
        "buffer_load_dwordx4 v[64:67], v1, s[16:19], s36 offen nt\n"
        "s_add_u32 s36, s36, s35\n"
        "buffer_load_dwordx4 v[68:71], v1, s[16:19], s36 offen nt\n"
        "s_add_u32 s36, s36, s35\n"
        "s_mov_b64 s[4:5], s[20:21]\n"
        "s_mov_b64 s[8:9], s[26:27]\n"
        "s_mov_b64 s[10:11], s[28:29]\n"
        "s_mov_b64 s[28:29], s[30:31]\n"
        "global_load_ushort v2, v2, s[4:5]\n"
        "global_load_dword v3, v16, s[10:11] sc1\n"
        "s_waitcnt lgkmcnt(0)\n"
        "s_lshl_b32 s48, s48, 4\n"
        "s_add_u32 s48, s48, s15\n"
        "s_branch .Lgeglu_ffn_decode_over\n"
        ".Lgeglu_ffn_decode_pace:\n"
        "s_add_u32 s48, s48, s58\n"
        ".Lgeglu_ffn_decode_pacel:\n"
        "s_memrealtime s[50:51]\n"
        "s_waitcnt lgkmcnt(0)\n"
        "s_lshl_b32 s50, s50, 4\n"
        "s_sub_u32 s50, s50, s48\n"
        "s_cmp_lt_i32 s50, 0\n"
        "s_cbranch_scc1 .Lgeglu_ffn_decode_pacel\n"
        "s_setpc_b64 s[60:61]\n"
        ".Lgeglu_ffn_decode_over:\n"
        "v_and_b32 v20, 1, v0\n"
        "v_lshlrev_b32 v20, 4, v20\n"
        "v_mov_b32 v21, 0xffff\n"
        "v_lshlrev_b32 v20, v20, v21\n"
        "v_bfe_u32 v21, v0, 1, 2\n"
        "v_cmp_eq_u32 s[40:41], 0, v21\n"
        "v_cmp_eq_u32 s[42:43], 1, v21\n"
        "v_cmp_eq_u32 s[44:45], 2, v21\n"
        "v_cmp_eq_u32 s[46:47], 3, v21\n"
        "v_and_b32 v14, 63, v0\n"
        "s_and_b32 s37, s3, 3\n"
        "s_lshl_b32 s37, s37, 9\n"
        "v_lshl_add_u32 v13, v14, 4, s37\n"
        "v_cndmask_b32 v4, 0, v20, s[40:41]\n"
        "v_cndmask_b32 v5, 0, v20, s[42:43]\n"
        "v_cndmask_b32 v6, 0, v20, s[44:45]\n"
        "v_cndmask_b32 v7, 0, v20, s[46:47]\n"
        "s_swappc_b64 s[60:61], s[62:63]\n"
        "buffer_load_dwordx4 v[72:75], v1, s[16:19], s36 offen nt\n"
        "s_add_u32 s36, s36, s35\n"
        "s_mov_b64 s[24:25], s[10:11]\n"
        "s_mov_b32 s26, 0x200000\n"
        "s_mov_b32 s27, s19\n"
        "s_add_u32 s52, s12, 1\n"
        "s_lshl_b32 s52, s52, 14\n"
        "s_lshl_b32 s37, s13, 8\n"
        "s_add_u32 s52, s52, s37\n"
        "s_add_u32 s53, s52, 8192\n"
        "v_and_b32 v19, 48, v0\n"
        "v_lshlrev_b32 v12, 7, v19\n"
        "v_lshl_add_u32 v12, v18, 4, v12\n"
        "v_and_b32 v31, 30, v0\n"
        "v_lshlrev_b32 v31, 4, v31\n"
        "v_bfe_u32 v20, v0, 5, 1\n"
        "v_lshl_add_u32 v31, v20, 4, v31\n"
        "v_and_b32 v20, 1, v0\n"
        "v_lshl_add_u32 v31, v20, 3, v31\n"
        "s_mov_b32 s49, 0xffffff00\n"
        "v_mov_b32 v17, 1\n"
        "s_swappc_b64 s[60:61], s[62:63]\n"
        "buffer_load_dwordx4 v[76:79], v1, s[16:19], s36 offen nt\n"
        "s_add_u32 s36, s36, s35\n"
        "s_swappc_b64 s[60:61], s[62:63]\n"
        "buffer_load_dwordx4 v[80:83], v1, s[16:19], s36 offen nt\n"
        "s_add_u32 s36, s36, s35\n"
        "s_swappc_b64 s[60:61], s[62:63]\n"
        "buffer_load_dwordx4 v[84:87], v1, s[16:19], s36 offen nt\n"
        "s_add_u32 s36, s36, s35\n"
        "s_swappc_b64 s[60:61], s[62:63]\n"
        "buffer_load_dwordx4 v[88:91], v1, s[16:19], s36 offen nt\n"
        "s_add_u32 s36, s36, s35\n"
        "s_swappc_b64 s[60:61], s[62:63]\n"
        "buffer_load_dwordx4 v[92:95], v1, s[16:19], s36 offen nt\n"
        "s_add_u32 s36, s36, s35\n"
        "s_swappc_b64 s[60:61], s[62:63]\n"
        "buffer_load_dwordx4 v[96:99], v1, s[16:19], s36 offen nt\n"
        "s_add_u32 s36, s36, s35\n"
        "s_swappc_b64 s[60:61], s[62:63]\n"
        "buffer_load_dwordx4 v[100:103], v1, s[16:19], s36 offen nt\n"
        "s_add_u32 s36, s36, s35\n"
        "s_swappc_b64 s[60:61], s[62:63]\n"
        "buffer_load_dwordx4 v[104:107], v1, s[16:19], s36 offen nt\n"
        "s_add_u32 s36, s36, s35\n"
        "s_barrier\n"
        "s_swappc_b64 s[60:61], s[62:63]\n"
        "buffer_load_dwordx4 v[108:111], v1, s[16:19], s36 offen nt\n"
        "s_add_u32 s36, s36, s35\n"
        "s_swappc_b64 s[60:61], s[62:63]\n"
        "buffer_load_dwordx4 v[112:115], v1, s[16:19], s36 offen nt\n"
        "s_add_u32 s36, s36, s35\n"
        "s_swappc_b64 s[60:61], s[62:63]\n"
        "buffer_load_dwordx4 v[116:119], v1, s[16:19], s36 offen nt\n"
        "s_add_u32 s36, s36, s35\n"
        "s_swappc_b64 s[60:61], s[62:63]\n"
        "buffer_load_dwordx4 v[120:123], v1, s[16:19], s36 offen nt\n"
        "s_add_u32 s36, s36, s35\n"
        "s_swappc_b64 s[60:61], s[62:63]\n"
        "buffer_load_dwordx4 v[124:127], v1, s[16:19], s36 offen nt\n"
        "s_waitcnt vmcnt(14)\n"
        "v_lshl_or_b32 v2, v2, 16, v2\n"
        "v_add_u32 v27, 0x9e3779b9, v3\n"
        "v_mov_b32 v29, v27\n"
        "v_and_b32_dpp v32, v2, v4 row_newbcast:0 row_mask:0xf bank_mask:0xf bound_ctrl:1\n"
        "v_and_b32_dpp v33, v2, v5 row_newbcast:0 row_mask:0xf bank_mask:0xf bound_ctrl:1\n"
        "v_and_b32_dpp v34, v2, v6 row_newbcast:0 row_mask:0xf bank_mask:0xf bound_ctrl:1\n"
        "v_and_b32_dpp v35, v2, v7 row_newbcast:0 row_mask:0xf bank_mask:0xf bound_ctrl:1\n"
        "v_and_b32_dpp v36, v2, v4 row_newbcast:1 row_mask:0xf bank_mask:0xf bound_ctrl:1\n"
        "v_and_b32_dpp v37, v2, v5 row_newbcast:1 row_mask:0xf bank_mask:0xf bound_ctrl:1\n"
        "v_and_b32_dpp v38, v2, v6 row_newbcast:1 row_mask:0xf bank_mask:0xf bound_ctrl:1\n"
        "v_and_b32_dpp v39, v2, v7 row_newbcast:1 row_mask:0xf bank_mask:0xf bound_ctrl:1\n"
        "v_and_b32_dpp v40, v2, v4 row_newbcast:2 row_mask:0xf bank_mask:0xf bound_ctrl:1\n"
        "v_and_b32_dpp v41, v2, v5 row_newbcast:2 row_mask:0xf bank_mask:0xf bound_ctrl:1\n"
        "v_and_b32_dpp v42, v2, v6 row_newbcast:2 row_mask:0xf bank_mask:0xf bound_ctrl:1\n"
        "v_and_b32_dpp v43, v2, v7 row_newbcast:2 row_mask:0xf bank_mask:0xf bound_ctrl:1\n"
        "v_and_b32_dpp v44, v2, v4 row_newbcast:3 row_mask:0xf bank_mask:0xf bound_ctrl:1\n"
        "v_and_b32_dpp v45, v2, v5 row_newbcast:3 row_mask:0xf bank_mask:0xf bound_ctrl:1\n"
        "v_and_b32_dpp v46, v2, v6 row_newbcast:3 row_mask:0xf bank_mask:0xf bound_ctrl:1\n"
        "v_and_b32_dpp v47, v2, v7 row_newbcast:3 row_mask:0xf bank_mask:0xf bound_ctrl:1\n"
        "v_and_b32_dpp v48, v2, v4 row_newbcast:4 row_mask:0xf bank_mask:0xf bound_ctrl:1\n"
        "v_and_b32_dpp v49, v2, v5 row_newbcast:4 row_mask:0xf bank_mask:0xf bound_ctrl:1\n"
        "v_and_b32_dpp v50, v2, v6 row_newbcast:4 row_mask:0xf bank_mask:0xf bound_ctrl:1\n"
        "v_and_b32_dpp v51, v2, v7 row_newbcast:4 row_mask:0xf bank_mask:0xf bound_ctrl:1\n"
        "v_and_b32_dpp v52, v2, v4 row_newbcast:5 row_mask:0xf bank_mask:0xf bound_ctrl:1\n"
        "v_and_b32_dpp v53, v2, v5 row_newbcast:5 row_mask:0xf bank_mask:0xf bound_ctrl:1\n"
        "v_and_b32_dpp v54, v2, v6 row_newbcast:5 row_mask:0xf bank_mask:0xf bound_ctrl:1\n"
        "v_and_b32_dpp v55, v2, v7 row_newbcast:5 row_mask:0xf bank_mask:0xf bound_ctrl:1\n"
        "v_and_b32_dpp v56, v2, v4 row_newbcast:6 row_mask:0xf bank_mask:0xf bound_ctrl:1\n"
        "v_and_b32_dpp v57, v2, v5 row_newbcast:6 row_mask:0xf bank_mask:0xf bound_ctrl:1\n"
        "v_and_b32_dpp v58, v2, v6 row_newbcast:6 row_mask:0xf bank_mask:0xf bound_ctrl:1\n"
        "v_and_b32_dpp v59, v2, v7 row_newbcast:6 row_mask:0xf bank_mask:0xf bound_ctrl:1\n"
        "v_and_b32_dpp v60, v2, v4 row_newbcast:7 row_mask:0xf bank_mask:0xf bound_ctrl:1\n"
        "v_and_b32_dpp v61, v2, v5 row_newbcast:7 row_mask:0xf bank_mask:0xf bound_ctrl:1\n"
        "v_and_b32_dpp v62, v2, v6 row_newbcast:7 row_mask:0xf bank_mask:0xf bound_ctrl:1\n"
        "v_and_b32_dpp v63, v2, v7 row_newbcast:7 row_mask:0xf bank_mask:0xf bound_ctrl:1\n"
        "v_mfma_f32_16x16x32_bf16 v[8:11], v[32:35], v[64:67], 0\n"
        "v_and_b32_dpp v32, v2, v4 row_newbcast:8 row_mask:0xf bank_mask:0xf bound_ctrl:1\n"
        "v_and_b32_dpp v33, v2, v5 row_newbcast:8 row_mask:0xf bank_mask:0xf bound_ctrl:1\n"
        "v_and_b32_dpp v34, v2, v6 row_newbcast:8 row_mask:0xf bank_mask:0xf bound_ctrl:1\n"
        "v_and_b32_dpp v35, v2, v7 row_newbcast:8 row_mask:0xf bank_mask:0xf bound_ctrl:1\n"
        "v_mfma_f32_16x16x32_bf16 v[8:11], v[36:39], v[68:71], v[8:11]\n"
        "v_and_b32_dpp v36, v2, v4 row_newbcast:9 row_mask:0xf bank_mask:0xf bound_ctrl:1\n"
        "v_and_b32_dpp v37, v2, v5 row_newbcast:9 row_mask:0xf bank_mask:0xf bound_ctrl:1\n"
        "v_and_b32_dpp v38, v2, v6 row_newbcast:9 row_mask:0xf bank_mask:0xf bound_ctrl:1\n"
        "v_and_b32_dpp v39, v2, v7 row_newbcast:9 row_mask:0xf bank_mask:0xf bound_ctrl:1\n"
        "s_waitcnt vmcnt(13)\n"
        "v_mfma_f32_16x16x32_bf16 v[8:11], v[40:43], v[72:75], v[8:11]\n"
        "v_and_b32_dpp v40, v2, v4 row_newbcast:10 row_mask:0xf bank_mask:0xf bound_ctrl:1\n"
        "v_and_b32_dpp v41, v2, v5 row_newbcast:10 row_mask:0xf bank_mask:0xf bound_ctrl:1\n"
        "v_and_b32_dpp v42, v2, v6 row_newbcast:10 row_mask:0xf bank_mask:0xf bound_ctrl:1\n"
        "v_and_b32_dpp v43, v2, v7 row_newbcast:10 row_mask:0xf bank_mask:0xf bound_ctrl:1\n"
        "s_waitcnt vmcnt(12)\n"
        "v_mfma_f32_16x16x32_bf16 v[8:11], v[44:47], v[76:79], v[8:11]\n"
        "v_and_b32_dpp v44, v2, v4 row_newbcast:11 row_mask:0xf bank_mask:0xf bound_ctrl:1\n"
        "v_and_b32_dpp v45, v2, v5 row_newbcast:11 row_mask:0xf bank_mask:0xf bound_ctrl:1\n"
        "v_and_b32_dpp v46, v2, v6 row_newbcast:11 row_mask:0xf bank_mask:0xf bound_ctrl:1\n"
        "v_and_b32_dpp v47, v2, v7 row_newbcast:11 row_mask:0xf bank_mask:0xf bound_ctrl:1\n"
        "s_waitcnt vmcnt(11)\n"
        "v_mfma_f32_16x16x32_bf16 v[8:11], v[48:51], v[80:83], v[8:11]\n"
        "v_and_b32_dpp v48, v2, v4 row_newbcast:12 row_mask:0xf bank_mask:0xf bound_ctrl:1\n"
        "v_and_b32_dpp v49, v2, v5 row_newbcast:12 row_mask:0xf bank_mask:0xf bound_ctrl:1\n"
        "v_and_b32_dpp v50, v2, v6 row_newbcast:12 row_mask:0xf bank_mask:0xf bound_ctrl:1\n"
        "v_and_b32_dpp v51, v2, v7 row_newbcast:12 row_mask:0xf bank_mask:0xf bound_ctrl:1\n"
        "s_waitcnt vmcnt(10)\n"
        "v_mfma_f32_16x16x32_bf16 v[8:11], v[52:55], v[84:87], v[8:11]\n"
        "v_and_b32_dpp v52, v2, v4 row_newbcast:13 row_mask:0xf bank_mask:0xf bound_ctrl:1\n"
        "v_and_b32_dpp v53, v2, v5 row_newbcast:13 row_mask:0xf bank_mask:0xf bound_ctrl:1\n"
        "v_and_b32_dpp v54, v2, v6 row_newbcast:13 row_mask:0xf bank_mask:0xf bound_ctrl:1\n"
        "v_and_b32_dpp v55, v2, v7 row_newbcast:13 row_mask:0xf bank_mask:0xf bound_ctrl:1\n"
        "s_waitcnt vmcnt(9)\n"
        "v_mfma_f32_16x16x32_bf16 v[8:11], v[56:59], v[88:91], v[8:11]\n"
        "v_and_b32_dpp v56, v2, v4 row_newbcast:14 row_mask:0xf bank_mask:0xf bound_ctrl:1\n"
        "v_and_b32_dpp v57, v2, v5 row_newbcast:14 row_mask:0xf bank_mask:0xf bound_ctrl:1\n"
        "v_and_b32_dpp v58, v2, v6 row_newbcast:14 row_mask:0xf bank_mask:0xf bound_ctrl:1\n"
        "v_and_b32_dpp v59, v2, v7 row_newbcast:14 row_mask:0xf bank_mask:0xf bound_ctrl:1\n"
        "s_waitcnt vmcnt(8)\n"
        "v_mfma_f32_16x16x32_bf16 v[8:11], v[60:63], v[92:95], v[8:11]\n"
        "v_and_b32_dpp v60, v2, v4 row_newbcast:15 row_mask:0xf bank_mask:0xf bound_ctrl:1\n"
        "v_and_b32_dpp v61, v2, v5 row_newbcast:15 row_mask:0xf bank_mask:0xf bound_ctrl:1\n"
        "v_and_b32_dpp v62, v2, v6 row_newbcast:15 row_mask:0xf bank_mask:0xf bound_ctrl:1\n"
        "v_and_b32_dpp v63, v2, v7 row_newbcast:15 row_mask:0xf bank_mask:0xf bound_ctrl:1\n"
        "s_waitcnt vmcnt(7)\n"
        "v_mfma_f32_16x16x32_bf16 v[8:11], v[32:35], v[96:99], v[8:11]\n"
        "s_waitcnt vmcnt(6)\n"
        "v_mfma_f32_16x16x32_bf16 v[8:11], v[36:39], v[100:103], v[8:11]\n"
        "s_waitcnt vmcnt(5)\n"
        "v_mfma_f32_16x16x32_bf16 v[8:11], v[40:43], v[104:107], v[8:11]\n"
        "s_waitcnt vmcnt(4)\n"
        "v_mfma_f32_16x16x32_bf16 v[8:11], v[44:47], v[108:111], v[8:11]\n"
        "s_waitcnt vmcnt(3)\n"
        "v_mfma_f32_16x16x32_bf16 v[8:11], v[48:51], v[112:115], v[8:11]\n"
        "s_waitcnt vmcnt(2)\n"
        "v_mfma_f32_16x16x32_bf16 v[8:11], v[52:55], v[116:119], v[8:11]\n"
        "s_waitcnt vmcnt(1)\n"
        "v_mfma_f32_16x16x32_bf16 v[8:11], v[56:59], v[120:123], v[8:11]\n"
        "s_waitcnt vmcnt(0)\n"
        "v_mfma_f32_16x16x32_bf16 v[8:11], v[60:63], v[124:127], v[8:11]\n"
        "s_mov_b32 exec_hi, 0\n"
        "s_nop 6\n"
        "ds_write_b128 v13, v[8:11]\n"
        "s_mov_b32 exec_lo, 1\n"
        "ds_add_rtn_u32 v2, v16, v17 offset:4096\n"
        "s_mov_b64 exec, -1\n"
        "ds_read2st64_b64 v[18:21], v31 offset0:0 offset1:1\n"
        "ds_read2st64_b64 v[22:25], v31 offset0:2 offset1:3\n"
        "s_waitcnt lgkmcnt(2)\n"
        "v_readfirstlane_b32 s37, v2\n"
        "s_cmpk_lg_u32 s37, 0x3\n"
        "s_cbranch_scc1 .Lgeglu_ffn_decode_end\n"
        "s_waitcnt lgkmcnt(1)\n"
        "v_pk_add_f32 v[18:19], v[18:19], v[20:21]\n"
        "s_waitcnt lgkmcnt(0)\n"
        "v_pk_add_f32 v[22:23], v[22:23], v[24:25]\n"
        "v_pk_add_f32 v[18:19], v[18:19], v[22:23]\n"
        "v_add_u32 v18, 0x80, v18\n"
        "v_add_u32 v19, 0x80, v19\n"
        "v_and_b32 v26, s49, v18\n"
        "v_and_b32 v28, s49, v19\n"
        "buffer_store_dwordx4 v[26:29], v12, s[24:27], s52 offen sc1\n"
        "buffer_store_dwordx4 v[26:29], v12, s[24:27], s53 offen sc1\n"
        ".Lgeglu_ffn_decode_end:\n"
        "s_endpgm\n"
        ".Lgeglu_ffn_decode_down:\n"
        "s_mov_b64 s[4:5], s[20:21]\n"
        "s_mov_b64 s[8:9], s[26:27]\n"
        "s_mov_b64 s[10:11], s[28:29]\n"
        "s_mov_b64 s[28:29], s[30:31]\n"
        "global_load_dword v3, v16, s[10:11] sc1\n"
        "v_and_b32 v19, 48, v0\n"
        "v_and_b32 v1, 0xf0, v0\n"
        "v_lshlrev_b32 v1, 7, v1\n"
        "v_lshl_add_u32 v1, v18, 4, v1\n"
        "s_mov_b64 s[16:17], s[8:9]\n"
        "s_mov_b64 s[24:25], s[10:11]\n"
        "s_mov_b32 s26, 0x200000\n"
        "s_mov_b32 s27, s19\n"
        "v_and_b32 v20, 1, v0\n"
        "v_lshlrev_b32 v20, 4, v20\n"
        "v_mov_b32 v21, 0xffff\n"
        "v_lshlrev_b32 v20, v20, v21\n"
        "v_bfe_u32 v21, v0, 1, 2\n"
        "v_cmp_eq_u32 s[40:41], 0, v21\n"
        "v_cmp_eq_u32 s[42:43], 1, v21\n"
        "v_cmp_eq_u32 s[44:45], 2, v21\n"
        "v_cmp_eq_u32 s[46:47], 3, v21\n"
        "v_and_b32 v14, 63, v0\n"
        "s_and_b32 s37, s3, 3\n"
        "s_lshl_b32 s37, s37, 9\n"
        "v_lshl_add_u32 v13, v14, 4, s37\n"
        "v_cndmask_b32 v4, 0, v20, s[40:41]\n"
        "v_cndmask_b32 v5, 0, v20, s[42:43]\n"
        "v_cndmask_b32 v6, 0, v20, s[44:45]\n"
        "v_cndmask_b32 v7, 0, v20, s[46:47]\n"
        "s_lshl_b32 s40, s12, 18\n"
        "s_lshl_b32 s38, s13, 8\n"
        "s_add_u32 s40, s40, s38\n"
        "v_lshrrev_b32 v12, 1, v19\n"
        "v_lshl_add_u32 v12, v18, 5, v12\n"
        "s_add_u32 s52, s12, 1\n"
        "s_lshl_b32 s52, s52, 14\n"
        "s_and_b32 s37, s3, 3\n"
        "s_lshl_b32 s37, s37, 11\n"
        "s_add_u32 s52, s52, s37\n"
        "s_getreg_b32 s38, hwreg(HW_REG_XCC_ID, 2, 1)\n"
        "s_lshl_b32 s38, s38, 13\n"
        "s_add_u32 s52, s52, s38\n"
        "s_and_b32 s41, s2, 7\n"
        "s_lshl_b32 s41, s41, 7\n"
        "s_add_u32 s41, s41, 1572864\n"
        "v_and_b32 v15, 3, v0\n"
        "v_lshlrev_b32 v15, 10, v15\n"
        "s_lshl_b32 s59, s13, 2\n"
        "s_add_u32 s59, s59, 32\n"
        "s_lshl_b32 s59, s59, 14\n"
        "s_lshl_b32 s37, s12, 7\n"
        "s_add_u32 s59, s59, s37\n"
        "v_lshlrev_b32 v31, 10, v19\n"
        "v_and_b32 v20, 8, v0\n"
        "v_lshl_add_u32 v31, v20, 9, v31\n"
        "v_and_b32 v20, 7, v0\n"
        "v_lshl_add_u32 v31, v20, 4, v31\n"
        "v_lshlrev_b32 v30, 3, v14\n"
        "s_movk_i32 s54, 0xff\n"
        "s_mov_b32 s55, 0xbdd2d3e8\n"
        "v_mov_b32 v17, 0xc0135761\n"
        "s_mov_b32 s39, 0\n"
        "s_waitcnt vmcnt(0) lgkmcnt(0)\n"
        "v_add_u32 v3, 0x9e3779b9, v3\n"
        "s_lshl_b32 s48, s48, 4\n"
        "v_readfirstlane_b32 s56, v3\n"
        "v_mov_b32 v27, v3\n"
        "v_mov_b32 v29, v3\n"
        "v_mov_b32 v3, 0x100\n"
        "s_mov_b32 s49, 0xffffff00\n"
        "s_add_u32 s48, s48, 2800\n"
        "s_add_u32 s48, s48, s15\n"
        "s_barrier\n"
        "s_swappc_b64 s[60:61], s[62:63]\n"
        "buffer_load_dwordx4 v[64:67], v1, s[16:19], s40 offen nt\n"
        "s_add_u32 s40, s40, 0x8000\n"
        "s_swappc_b64 s[60:61], s[62:63]\n"
        "buffer_load_dwordx4 v[68:71], v1, s[16:19], s40 offen nt\n"
        "s_add_u32 s40, s40, 0x8000\n"
        "s_swappc_b64 s[60:61], s[62:63]\n"
        "buffer_load_dwordx4 v[72:75], v1, s[16:19], s40 offen nt\n"
        "s_add_u32 s40, s40, 0x8000\n"
        "buffer_load_dwordx2 v[18:19], v15, s[24:27], s41 offen sc1\n"
        "s_swappc_b64 s[60:61], s[62:63]\n"
        "buffer_load_dwordx4 v[76:79], v1, s[16:19], s40 offen nt\n"
        "s_add_u32 s40, s40, 0x8000\n"
        "s_swappc_b64 s[60:61], s[62:63]\n"
        "buffer_load_dwordx4 v[80:83], v1, s[16:19], s40 offen nt\n"
        "s_add_u32 s40, s40, 0x8000\n"
        "s_swappc_b64 s[60:61], s[62:63]\n"
        "buffer_load_dwordx4 v[84:87], v1, s[16:19], s40 offen nt\n"
        "s_add_u32 s40, s40, 0x8000\n"
        "s_swappc_b64 s[60:61], s[62:63]\n"
        "buffer_load_dwordx4 v[88:91], v1, s[16:19], s40 offen nt\n"
        "s_add_u32 s40, s40, 0x8000\n"
        "s_swappc_b64 s[60:61], s[62:63]\n"
        "buffer_load_dwordx4 v[92:95], v1, s[16:19], s40 offen nt\n"
        "s_waitcnt vmcnt(5)\n"
        "v_cmp_eq_u32 s[44:45], s56, v19\n"
        "v_and_b32 v18, 1, v18\n"
        "v_cmp_ne_u32 s[46:47], 0, v18\n"
        "s_nop 1\n"
        "s_and_b32 s46, s46, s44\n"
        "s_bcnt1_i32_b32 s44, s44\n"
        "s_bcnt1_i32_b32 s46, s46\n"
        "s_lshl_b32 s46, s46, 1\n"
        "s_cmp_ge_u32 s46, s44\n"
        "s_cselect_b32 s42, 8192, 0\n"
        "s_xor_b32 s52, s52, s42\n"
        "s_waitcnt vmcnt(1)\n"
        ".Lgeglu_ffn_decode_poll1:\n"
        "buffer_load_dwordx2 v[96:97], v12, s[24:27], s52 offen offset:0 sc1\n"
        "buffer_load_dwordx2 v[98:99], v12, s[24:27], s52 offen offset:512 sc1\n"
        "buffer_load_dwordx2 v[100:101], v12, s[24:27], s52 offen offset:1024 sc1\n"
        "buffer_load_dwordx2 v[102:103], v12, s[24:27], s52 offen offset:1536 sc1\n"
        "s_addk_i32 s39, 1\n"
        "s_cmp_lt_u32 s39, 1048576\n"
        "s_cselect_b64 s[44:45], -1, 0\n"
        "s_waitcnt vmcnt(2)\n"
        "v_add_f32 v20, v96, v98\n"
        "v_bitop3_b32 v22, v97, v99, s56 bitop3:0x7e\n"
        "s_waitcnt vmcnt(1)\n"
        "v_add_f32 v20, v20, v100\n"
        "v_or3_b32 v23, v96, v98, v100\n"
        "s_waitcnt vmcnt(0)\n"
        "v_add_f32 v20, v20, v102\n"
        "v_cvt_pk_bf16_f32 v20, v16, v20\n"
        "v_mul_f32 v18, v20, v20\n"
        "v_fma_f32 v18, v18, s55, v17\n"
        "v_mul_f32 v18, v18, v20\n"
        "v_exp_f32 v18, v18\n"
        "v_mov_b32_dpp v19, v20 row_ror:8 row_mask:0xf bank_mask:0xf bound_ctrl:1\n"
        "v_add_f32 v18, 1.0, v18\n"
        "v_rcp_f32 v18, v18\n"
        "v_bitop3_b32 v24, v101, v103, s56 bitop3:0x7e\n"
        "v_mul_f32 v18, v18, v20\n"
        "v_cvt_pk_bf16_f32 v18, v16, v18\n"
        "v_mul_f32 v18, v18, v19\n"
        "v_cvt_pk_bf16_f32 v2, v18, v18\n"
        "v_bitop3_b32 v23, v23, v102, s54 bitop3:0xa8\n"
        "v_or3_b32 v22, v22, v24, v23\n"
        "v_and_b32_dpp v32, v2, v4 row_newbcast:0 row_mask:0xf bank_mask:0xf bound_ctrl:1\n"
        "v_and_b32_dpp v33, v2, v5 row_newbcast:0 row_mask:0xf bank_mask:0xf bound_ctrl:1\n"
        "v_and_b32_dpp v34, v2, v6 row_newbcast:0 row_mask:0xf bank_mask:0xf bound_ctrl:1\n"
        "v_and_b32_dpp v35, v2, v7 row_newbcast:0 row_mask:0xf bank_mask:0xf bound_ctrl:1\n"
        "v_cmp_ne_u32 vcc, 0, v22\n"
        "s_and_b64 vcc, vcc, s[44:45]\n"
        "s_cbranch_vccnz .Lgeglu_ffn_decode_poll1\n"
        "v_mfma_f32_16x16x32_bf16 v[8:11], v[32:35], v[64:67], 0\n"
        "v_and_b32_dpp v36, v2, v4 row_newbcast:1 row_mask:0xf bank_mask:0xf bound_ctrl:1\n"
        "v_and_b32_dpp v37, v2, v5 row_newbcast:1 row_mask:0xf bank_mask:0xf bound_ctrl:1\n"
        "v_and_b32_dpp v38, v2, v6 row_newbcast:1 row_mask:0xf bank_mask:0xf bound_ctrl:1\n"
        "v_and_b32_dpp v39, v2, v7 row_newbcast:1 row_mask:0xf bank_mask:0xf bound_ctrl:1\n"
        "v_and_b32_dpp v40, v2, v4 row_newbcast:2 row_mask:0xf bank_mask:0xf bound_ctrl:1\n"
        "v_and_b32_dpp v41, v2, v5 row_newbcast:2 row_mask:0xf bank_mask:0xf bound_ctrl:1\n"
        "v_and_b32_dpp v42, v2, v6 row_newbcast:2 row_mask:0xf bank_mask:0xf bound_ctrl:1\n"
        "v_and_b32_dpp v43, v2, v7 row_newbcast:2 row_mask:0xf bank_mask:0xf bound_ctrl:1\n"
        "v_mfma_f32_16x16x32_bf16 v[8:11], v[36:39], v[68:71], v[8:11]\n"
        "v_and_b32_dpp v44, v2, v4 row_newbcast:3 row_mask:0xf bank_mask:0xf bound_ctrl:1\n"
        "v_and_b32_dpp v45, v2, v5 row_newbcast:3 row_mask:0xf bank_mask:0xf bound_ctrl:1\n"
        "v_and_b32_dpp v46, v2, v6 row_newbcast:3 row_mask:0xf bank_mask:0xf bound_ctrl:1\n"
        "v_and_b32_dpp v47, v2, v7 row_newbcast:3 row_mask:0xf bank_mask:0xf bound_ctrl:1\n"
        "v_mfma_f32_16x16x32_bf16 v[8:11], v[40:43], v[72:75], v[8:11]\n"
        "v_and_b32_dpp v48, v2, v4 row_newbcast:4 row_mask:0xf bank_mask:0xf bound_ctrl:1\n"
        "v_and_b32_dpp v49, v2, v5 row_newbcast:4 row_mask:0xf bank_mask:0xf bound_ctrl:1\n"
        "v_and_b32_dpp v50, v2, v6 row_newbcast:4 row_mask:0xf bank_mask:0xf bound_ctrl:1\n"
        "v_and_b32_dpp v51, v2, v7 row_newbcast:4 row_mask:0xf bank_mask:0xf bound_ctrl:1\n"
        "v_mfma_f32_16x16x32_bf16 v[8:11], v[44:47], v[76:79], v[8:11]\n"
        "v_and_b32_dpp v52, v2, v4 row_newbcast:5 row_mask:0xf bank_mask:0xf bound_ctrl:1\n"
        "v_and_b32_dpp v53, v2, v5 row_newbcast:5 row_mask:0xf bank_mask:0xf bound_ctrl:1\n"
        "v_and_b32_dpp v54, v2, v6 row_newbcast:5 row_mask:0xf bank_mask:0xf bound_ctrl:1\n"
        "v_and_b32_dpp v55, v2, v7 row_newbcast:5 row_mask:0xf bank_mask:0xf bound_ctrl:1\n"
        "v_mfma_f32_16x16x32_bf16 v[8:11], v[48:51], v[80:83], v[8:11]\n"
        "v_and_b32_dpp v56, v2, v4 row_newbcast:6 row_mask:0xf bank_mask:0xf bound_ctrl:1\n"
        "v_and_b32_dpp v57, v2, v5 row_newbcast:6 row_mask:0xf bank_mask:0xf bound_ctrl:1\n"
        "v_and_b32_dpp v58, v2, v6 row_newbcast:6 row_mask:0xf bank_mask:0xf bound_ctrl:1\n"
        "v_and_b32_dpp v59, v2, v7 row_newbcast:6 row_mask:0xf bank_mask:0xf bound_ctrl:1\n"
        "v_mfma_f32_16x16x32_bf16 v[8:11], v[52:55], v[84:87], v[8:11]\n"
        "v_and_b32_dpp v60, v2, v4 row_newbcast:7 row_mask:0xf bank_mask:0xf bound_ctrl:1\n"
        "v_and_b32_dpp v61, v2, v5 row_newbcast:7 row_mask:0xf bank_mask:0xf bound_ctrl:1\n"
        "v_and_b32_dpp v62, v2, v6 row_newbcast:7 row_mask:0xf bank_mask:0xf bound_ctrl:1\n"
        "v_and_b32_dpp v63, v2, v7 row_newbcast:7 row_mask:0xf bank_mask:0xf bound_ctrl:1\n"
        "v_mfma_f32_16x16x32_bf16 v[8:11], v[56:59], v[88:91], v[8:11]\n"
        "s_nop 0\n"
        "v_mfma_f32_16x16x32_bf16 v[8:11], v[60:63], v[92:95], v[8:11]\n"
        "s_mov_b32 exec_hi, 0\n"
        "s_mov_b32 s52, s59\n"
        "s_add_u32 s53, s59, 8192\n"
        "s_nop 4\n"
        "ds_write_b128 v13, v[8:11] offset:2048\n"
        "s_mov_b32 exec_lo, 1\n"
        "ds_add_rtn_u32 v2, v16, v3 offset:4096\n"
        "s_mov_b64 exec, -1\n"
        "ds_read2st64_b64 v[18:21], v30 offset0:4 offset1:5\n"
        "ds_read2st64_b64 v[22:25], v30 offset0:6 offset1:7\n"
        "s_waitcnt lgkmcnt(2)\n"
        "v_readfirstlane_b32 s37, v2\n"
        "s_cmpk_lg_u32 s37, 0x304\n"
        "s_cbranch_scc1 .Lgeglu_ffn_decode_end2\n"
        "s_waitcnt lgkmcnt(1)\n"
        "v_pk_add_f32 v[18:19], v[18:19], v[20:21]\n"
        "s_waitcnt lgkmcnt(0)\n"
        "v_pk_add_f32 v[22:23], v[22:23], v[24:25]\n"
        "v_pk_add_f32 v[18:19], v[18:19], v[22:23]\n"
        "v_add_u32 v18, 0x80, v18\n"
        "v_add_u32 v19, 0x80, v19\n"
        "v_and_b32 v26, s49, v18\n"
        "v_and_b32 v28, s49, v19\n"
        "buffer_store_dwordx4 v[26:29], v31, s[24:27], s52 offen sc1\n"
        "buffer_store_dwordx4 v[26:29], v31, s[24:27], s53 offen sc1\n"
        ".Lgeglu_ffn_decode_end2:\n"
        "s_endpgm\n"
        ".Lgeglu_ffn_decode_service:\n"
        "s_cmp_gt_u32 s3, 1\n"
        "s_cbranch_scc1 .Lgeglu_ffn_decode_last\n"
        "s_sub_u32 s12, s2, 224\n"
        "v_and_b32 v14, 63, v0\n"
        "v_lshlrev_b32 v12, 4, v14\n"
        "s_getreg_b32 s38, hwreg(HW_REG_XCC_ID, 2, 1)\n"
        "s_add_u32 s40, s12, 64\n"
        "s_lshl_b32 s40, s40, 14\n"
        "s_and_b32 s37, s12, 16\n"
        "s_lshl_b32 s37, s37, 8\n"
        "s_add_u32 s40, s40, s37\n"
        "s_bfe_u32 s13, s12, 0x10003\n"
        "s_lshl_b32 s37, s13, 13\n"
        "s_add_u32 s40, s40, s37\n"
        "s_xor_b32 s41, s40, 8192\n"
        "v_mov_b32 v4, 0\n"
        "v_mov_b32 v5, 0\n"
        "v_mov_b32 v6, 0\n"
        "v_mov_b32 v7, 0\n"
        "s_cmp_lg_u32 s3, 0\n"
        "s_waitcnt lgkmcnt(0)\n"
        "s_mov_b64 s[4:5], s[20:21]\n"
        "s_mov_b64 s[8:9], s[26:27]\n"
        "s_mov_b64 s[10:11], s[28:29]\n"
        "s_mov_b64 s[28:29], s[30:31]\n"
        "s_memrealtime s[48:49]\n"
        "s_mov_b64 s[24:25], s[10:11]\n"
        "s_mov_b32 s26, 0x200000\n"
        "s_mov_b32 s27, s19\n"
        "s_cbranch_scc1 .Lgeglu_ffn_decode_noprobe\n"
        "s_mov_b64 exec, 0xff\n"
        "buffer_store_dwordx4 v[4:7], v12, s[24:27], s40 offen sc1\n"
        "buffer_store_dwordx4 v[4:7], v12, s[24:27], s41 offen sc1\n"
        "s_mov_b64 exec, -1\n"
        "buffer_load_dword v9, v16, s[24:27], 0 offen sc1\n"
        "s_waitcnt vmcnt(2)\n"
        "s_memtime s[44:45]\n"
        "s_waitcnt vmcnt(1)\n"
        "s_memtime s[46:47]\n"
        "s_waitcnt lgkmcnt(0)\n"
        "s_sub_u32 s44, s46, s44\n"
        "s_cmpk_gt_u32 s44, 100\n"
        "s_cselect_b32 s44, 0, 1\n"
        "s_xor_b32 s43, s44, s13\n"
        "s_xor_b32 s44, s43, s38\n"
        "v_mov_b32 v8, s44\n"
        "s_mov_b32 s36, 1572864\n"
        "s_lshl_b32 s37, s12, 7\n"
        "s_add_u32 s42, s36, s37\n"
        "v_and_b32 v18, 31, v0\n"
        "v_lshlrev_b32 v18, 7, v18\n"
        "s_waitcnt vmcnt(0)\n"
        "v_add_u32 v9, 0x9e3779b9, v9\n"
        "v_lshlrev_b32 v10, 3, v14\n"
        "s_mov_b64 exec, 0xffff\n"
        "buffer_store_dwordx2 v[8:9], v10, s[24:27], s42 offen sc1\n"
        "s_mov_b64 exec, -1\n"
        "s_mov_b32 s59, s48\n"
        "s_lshl_b32 s48, s48, 4\n"
        "s_movk_i32 s58, 3200\n"
        "s_swappc_b64 s[60:61], s[62:63]\n"
        "s_mov_b32 s48, s59\n"
        "s_mov_b32 s39, 0\n"
        ".Lgeglu_ffn_decode_pollv:\n"
        "buffer_load_dwordx2 v[20:21], v18, s[24:27], s36 offen sc1\n"
        "s_addk_i32 s39, 1\n"
        "s_cmp_lt_u32 s39, 1048576\n"
        "s_cselect_b64 s[44:45], -1, 0\n"
        "s_waitcnt vmcnt(0)\n"
        "v_cmp_ne_u32 vcc, v9, v21\n"
        "v_and_b32 v22, 1, v20\n"
        "v_cmp_ne_u32 s[46:47], 0, v22\n"
        "s_and_b64 vcc, vcc, s[44:45]\n"
        "s_cbranch_vccnz .Lgeglu_ffn_decode_pollv\n"
        "s_bcnt1_i32_b32 s46, s46\n"
        "s_cmp_ge_u32 s46, 16\n"
        "s_cselect_b32 s46, 8192, 0\n"
        "v_mov_b32 v8, s46\n"
        "ds_write_b64 v16, v[8:9] offset:4104\n"
        ".Lgeglu_ffn_decode_noprobe:\n"
        "s_waitcnt lgkmcnt(0)\n"
        "s_barrier\n"
        "ds_read_b64 v[26:27], v16 offset:4104\n"
        "s_waitcnt lgkmcnt(0)\n"
        "v_readfirstlane_b32 s57, v26\n"
        "v_readfirstlane_b32 s56, v27\n"
        "s_lshl_b32 s13, s12, 1\n"
        "s_add_u32 s13, s13, s3\n"
        "s_movk_i32 s30, 2048\n"
        "s_mov_b32 s31, s27\n"
        "s_lshr_b32 s37, s13, 3\n"
        "s_lshl_b32 s37, s37, 8\n"
        "s_and_b32 s36, s13, 3\n"
        "s_lshl_b32 s36, s36, 6\n"
        "s_add_u32 s37, s37, s36\n"
        "s_and_b32 s36, s13, 4\n"
        "s_lshl_b32 s36, s36, 1\n"
        "s_add_u32 s37, s37, s36\n"
        "v_bfe_u32 v15, v0, 1, 2\n"
        "v_lshlrev_b32 v15, 4, v15\n"
        "v_and_b32 v18, 1, v0\n"
        "v_lshl_add_u32 v15, v18, 2, v15\n"
        "v_lshrrev_b32 v18, 5, v14\n"
        "v_lshl_add_u32 v15, v18, 1, v15\n"
        "v_add_u32 v15, s37, v15\n"
        "s_movk_i32 s46, 0xff\n"
        "s_movk_i32 s47, 0xff\n"
        "s_add_u32 s53, s12, 32\n"
        "s_lshl_b32 s53, s53, 14\n"
        "s_lshl_b32 s37, s3, 12\n"
        "s_add_u32 s53, s53, s37\n"
        "s_lshl_b32 s38, s38, 13\n"
        "s_add_u32 s53, s53, s38\n"
        "s_xor_b32 s53, s53, s57\n"
        "s_movk_i32 s54, 0xff\n"
        "s_mov_b32 s39, 0\n"
        "v_mov_b32 v44, 0\n"
        "v_mov_b32 v45, v27\n"
        "v_mov_b32 v46, 0\n"
        "v_mov_b32 v47, v27\n"
        "s_lshl_b32 s48, s48, 4\n"
        "s_movk_i32 s58, 4800\n"
        "s_swappc_b64 s[60:61], s[62:63]\n"
        "s_mov_b64 exec, s[46:47]\n"
        "buffer_store_short v16, v15, s[28:31], 0 offen sc1\n"
        "s_mov_b64 exec, -1\n"
        ".Lgeglu_ffn_decode_poll2:\n"
        "buffer_load_dwordx4 v[32:35], v12, s[24:27], s53 offen offset:0 sc1\n"
        "buffer_load_dwordx4 v[36:39], v12, s[24:27], s53 offen offset:1024 sc1\n"
        "buffer_load_dwordx4 v[40:43], v12, s[24:27], s53 offen offset:2048 sc1\n"
        "s_mov_b32 exec_hi, 0\n"
        "buffer_load_dwordx4 v[44:47], v12, s[24:27], s53 offen offset:3072 sc1\n"
        "s_mov_b32 exec_hi, -1\n"
        "s_addk_i32 s39, 1\n"
        "s_cmp_lt_u32 s39, 1048576\n"
        "s_cselect_b64 s[44:45], -1, 0\n"
        "s_waitcnt vmcnt(3)\n"
        "v_bitop3_b32 v18, v33, v35, s56 bitop3:0x7e\n"
        "s_waitcnt vmcnt(2)\n"
        "v_bitop3_b32 v19, v37, v39, s56 bitop3:0x7e\n"
        "v_or3_b32 v20, v32, v34, v36\n"
        "v_add_f32 v22, v32, v36\n"
        "v_add_f32 v23, v34, v38\n"
        "s_waitcnt vmcnt(1)\n"
        "v_bitop3_b32 v21, v41, v43, s56 bitop3:0x7e\n"
        "v_or3_b32 v20, v20, v38, v40\n"
        "v_or3_b32 v18, v18, v19, v21\n"
        "v_add_f32 v22, v22, v40\n"
        "v_add_f32 v23, v23, v42\n"
        "s_waitcnt vmcnt(0)\n"
        "v_or3_b32 v20, v20, v42, v44\n"
        "v_bitop3_b32 v19, v45, v47, s56 bitop3:0x7e\n"
        "v_bitop3_b32 v20, v20, v46, s54 bitop3:0xa8\n"
        "v_add_f32 v22, v22, v44\n"
        "v_or3_b32 v18, v18, v19, v20\n"
        "v_add_f32 v23, v23, v46\n"
        "v_cmp_ne_u32 vcc, 0, v18\n"
        "s_and_b64 vcc, vcc, s[44:45]\n"
        "s_cbranch_vccnz .Lgeglu_ffn_decode_poll2\n"
        "v_permlane32_swap_b32 v22, v23\n"
        "v_add_f32 v22, v22, v23\n"
        "v_mov_b32 v23, v22\n"
        "s_nop 1\n"
        "v_permlane16_swap_b32 v22, v23\n"
        "v_add_f32 v22, v22, v23\n"
        "s_cmp_lg_u32 s13, 0\n"
        "s_nop 0\n"
        "v_add_f32_dpp v22, v22, v22 row_ror:8 row_mask:0xf bank_mask:0xf bound_ctrl:1\n"
        "s_mov_b64 exec, s[46:47]\n"
        "v_cvt_pk_bf16_f32 v22, v22, v22\n"
        "buffer_store_short v22, v15, s[28:31], 0 offen sc1\n"
        "s_cbranch_scc1 .Lgeglu_ffn_decode_end7\n"
        "s_mov_b64 exec, 1\n"
        "buffer_store_dword v27, v16, s[24:27], 0 offen sc1\n"
        ".Lgeglu_ffn_decode_end7:\n"
        ".Lgeglu_ffn_decode_last:\n"
        "s_endpgm\n"
        :
        : "{s[0:1]}"(__builtin_amdgcn_kernarg_segment_ptr()), "{s2}"(blockIdx.x), "{v0}"(threadIdx.x)
        : "v1", "v2", "v3", "v4", "v5", "v6", "v7", "v8", "v9", "v10", "v11", "v12", "v13", "v14", "v15", "v16",
          "v17", "v18", "v19", "v20", "v21", "v22", "v23", "v24", "v25", "v26", "v27", "v28", "v29", "v30", "v31", "v32",
          "v33", "v34", "v35", "v36", "v37", "v38", "v39", "v40", "v41", "v42", "v43", "v44", "v45", "v46", "v47", "v48",
          "v49", "v50", "v51", "v52", "v53", "v54", "v55", "v56", "v57", "v58", "v59", "v60", "v61", "v62", "v63", "v64",
          "v65", "v66", "v67", "v68", "v69", "v70", "v71", "v72", "v73", "v74", "v75", "v76", "v77", "v78", "v79", "v80",
          "v81", "v82", "v83", "v84", "v85", "v86", "v87", "v88", "v89", "v90", "v91", "v92", "v93", "v94", "v95", "v96",
          "v97", "v98", "v99", "v100", "v101", "v102", "v103", "v104", "v105", "v106", "v107", "v108", "v109", "v110", "v111", "v112",
          "v113", "v114", "v115", "v116", "v117", "v118", "v119", "v120", "v121", "v122", "v123", "v124", "v125", "v126", "v127", "s3",
          "s4", "s5", "s6", "s7", "s8", "s9", "s10", "s11", "s12", "s13", "s14", "s15", "s16", "s17", "s18", "s19",
          "s20", "s21", "s22", "s23", "s24", "s25", "s26", "s27", "s28", "s29", "s30", "s31", "s35", "s36", "s37", "s38",
          "s39", "s40", "s41", "s42", "s43", "s44", "s45", "s46", "s47", "s48", "s49", "s50", "s51", "s52", "s53", "s54",
          "s55", "s56", "s57", "s58", "s59", "s60", "s61", "s62", "s63", "vcc", "scc", "memory");
    asm volatile("" :: "v"((__attribute__((address_space(3))) unsigned*)lds));
}

extern "C" void kernel_launch(void* const* d_in, const int* in_sizes, int n_in, void* d_out, int out_size, void* d_ws,
                              size_t ws_size, hipStream_t stream) {
    geglu_ffn_decode<<<256, 512, 0, stream>>>(d_in[0], d_in[1], d_in[2], d_in[3], d_ws, d_out);
}
```
